```python
import torch

def _softmax(x, axis=-1):
    return torch.softmax(x, dim=axis)
def _gelu(x):
    return torch.nn.functional.gelu(x, approximate="tanh")
def _silu(x):
    return torch.nn.functional.silu(x)
def _sigmoid(x):
    return torch.sigmoid(x)
def _layernorm(x, axis=-1, eps=1e-5):
    m = x.mean(dim=axis, keepdim=True)
    v = x.var(dim=axis, keepdim=True, unbiased=False)
    return (x - m) / torch.sqrt(v + eps)
def _scatter_add(tgt, idx, src, axis=0):
    return tgt.clone().index_add_(axis, idx, src)
def _segment_sum(src, seg, n):
    out = torch.zeros((n,) + src.shape[1:], dtype=src.dtype)
    return out.index_add_(0, seg, src)
def _pad(x, pw):
    flat = tuple(v for pair in reversed(pw) for v in pair)
    return torch.nn.functional.pad(x, flat)

kernel_name = 'sequential_preLN__linear_kernel_hedgehog__swiglu_ffn__prompt'

def setup_inputs():

    _w = (lambda a: (lambda t: t.to(torch.bfloat16) if t.is_floating_point() else t)(torch.as_tensor(a)))
    def _A_setup():
        B, S, D, H = 1, 2048, 512, 32
        Dh = D // H
        return {
            "x": _w((lambda *s: torch.randn(*s, dtype=torch.bfloat16) * 0.05)(B, S, D)),
            "wqkv": _w((lambda *s: torch.randn(*s, dtype=torch.bfloat16) * 0.2)(D, 3 * D)),
            "w_phi": _w((lambda *s: torch.randn(*s, dtype=torch.bfloat16) * 0.2)(H, Dh, Dh) * (Dh ** -0.5)),
            "wo": _w((lambda *s: torch.randn(*s, dtype=torch.bfloat16) * 0.05)(D, D)),
        }
    _A_in = _A_setup()

    def _B_setup():
        B, S, D, F = 1, 2048, 512, 1792
        return {
            "x": _w((lambda *s: torch.randn(*s, dtype=torch.bfloat16) * 0.05)(B, S, D)),
            "w_gate": _w((lambda *s: torch.randn(*s, dtype=torch.bfloat16) * 0.05)(D, F)),
            "w_up": _w((lambda *s: torch.randn(*s, dtype=torch.bfloat16) * 0.05)(D, F)),
            "w_down": _w((lambda *s: torch.randn(*s, dtype=torch.bfloat16) * 0.05)(F, D)),
        }
    _B_in = _B_setup()

    return {
        "A_x": _A_in["x"],
        "A_wqkv": _A_in["wqkv"],
        "A_w_phi": _A_in["w_phi"],
        "A_wo": _A_in["wo"],
        "B_x": _B_in["x"],
        "B_w_gate": _B_in["w_gate"],
        "B_w_up": _B_in["w_up"],
        "B_w_down": _B_in["w_down"],
    }

def reference(A_x, A_wqkv, A_w_phi, A_wo, B_x, B_w_gate, B_w_up, B_w_down):
    def _A_ref(x, wqkv, w_phi, wo):
        B, S, D = x.shape
        H = 32; Dh = D // H
        qkv = (lambda x, *d: x.permute(*d))((x @ wqkv).reshape(B, S, 3, H, Dh), 2, 0, 3, 1, 4)
        q, k, v = qkv[0], qkv[1], qkv[2]
        pq = torch.einsum("bhsd,hdf->bhsf", q, w_phi)
        pk = torch.einsum("bhsd,hdf->bhsf", k, w_phi)
        phi_q = torch.concatenate([_softmax(pq, axis=-1), _softmax(-pq, axis=-1)], axis=-1)
        phi_k = torch.concatenate([_softmax(pk, axis=-1), _softmax(-pk, axis=-1)], axis=-1)
        kv = torch.einsum("bhsf,bhsd->bhfd", phi_k, v)
        z  = torch.einsum("bhsf->bhf", phi_k)
        num = torch.einsum("bhqf,bhfd->bhqd", phi_q, kv)
        den = torch.einsum("bhqf,bhf->bhq", phi_q, z)[..., None] + 1e-6
        o = num / den
        return (lambda x, *d: x.permute(*d))(o, 0, 2, 1, 3).reshape(B, S, D) @ wo

    def _B_ref(x, w_gate, w_up, w_down):
        g = _silu(x @ w_gate)
        u = x @ w_up
        return (g * u) @ w_down

    x = A_x
    h = x + _A_ref(_layernorm(x, axis=-1), A_wqkv, A_w_phi, A_wo)
    h2 = h + _B_ref(_layernorm(h, axis=-1), B_w_gate, B_w_up, B_w_down)
    return h2
```

```cpp
#include <hip/hip_runtime.h>
#include <hip/hip_bf16.h>
#include <stdint.h>

typedef __hip_bfloat16 bf16;
typedef __attribute__((ext_vector_type(8))) short bf16x8;
typedef __attribute__((ext_vector_type(4))) short bf16x4;
typedef __attribute__((ext_vector_type(4))) float f32x4;
typedef __attribute__((ext_vector_type(4))) unsigned int u32x4;
typedef __attribute__((ext_vector_type(2))) unsigned int u32x2;
typedef __attribute__((ext_vector_type(8))) __bf16 bfv8;
typedef __attribute__((ext_vector_type(4))) __bf16 bfv4;
#define LAS __attribute__((address_space(3)))

constexpr int S_ = 2048, D_ = 512, H_ = 32, F_ = 1792;

constexpr size_t WS_PHIQ = 0;
constexpr size_t WS_SLAB = 4u << 20;
constexpr size_t WS_ZSLAB = WS_SLAB + (size_t)32 * 32 * 32 * 16 * 4;
constexpr size_t WS_KVT = 8u << 20;
constexpr size_t WS_H = 9u << 20;
constexpr size_t WS_STAT = 11u << 20;
constexpr size_t WS_ACT = 12u << 20;

__device__ __forceinline__ f32x4 mfma32(bf16x8 a, bf16x8 b, f32x4 c) {
  return __builtin_amdgcn_mfma_f32_16x16x32_bf16(__builtin_bit_cast(bfv8, a), __builtin_bit_cast(bfv8, b), c, 0, 0, 0);
}
__device__ __forceinline__ f32x4 mfma16(bf16x4 a, bf16x4 b, f32x4 c) {
  return __builtin_amdgcn_mfma_f32_16x16x16bf16_1k(a, b, c, 0, 0, 0);
}
__device__ __forceinline__ unsigned pk_bf16(float lo, float hi) {
  typedef __attribute__((ext_vector_type(2))) __bf16 bf2;
  typedef __attribute__((ext_vector_type(2))) float f2;
  f2 v = {lo, hi};
  bf2 r = __builtin_convertvector(v, bf2);
  return __builtin_bit_cast(unsigned, r);
}
__device__ __forceinline__ float rbf(float x) {
  unsigned u = pk_bf16(x, 0.f);
  return __uint_as_float(u << 16);
}
__device__ __forceinline__ float bf_lo(unsigned u) { return __uint_as_float(u << 16); }
__device__ __forceinline__ float bf_hi(unsigned u) { return __uint_as_float(u & 0xffff0000u); }

__device__ __forceinline__ void glds16(const void* g, unsigned lds_base) {
  unsigned keep;
  const unsigned b = __builtin_amdgcn_readfirstlane(lds_base);
  asm volatile("s_mov_b32 %0, m0\n\ts_mov_b32 m0, %2\n\ts_nop 0\n\tglobal_load_lds_dwordx4 %1, off\n\ts_mov_b32 m0, %0"
               : "=&s"(keep) : "v"(g), "s"(b) : "memory");
}
__device__ __forceinline__ unsigned lds_addr(const void* p) { return (unsigned)(size_t)(LAS const char*)p; }
__device__ __forceinline__ bf16x4 tr_read(const char* p) {
  return __builtin_amdgcn_ds_read_tr16_b64_v4i16((LAS bf16x4*)p);
}
template <int N> __device__ __forceinline__ void wait_vm() { asm volatile("s_waitcnt vmcnt(%0)" ::"n"(N) : "memory"); }
__device__ __forceinline__ void wait_vm_dyn(int n) {
  switch (n) {
    case 0: wait_vm<0>(); break;
    case 1: wait_vm<1>(); break;
    case 2: wait_vm<2>(); break;
    case 3: wait_vm<3>(); break;
    case 4: wait_vm<4>(); break;
    case 6: wait_vm<6>(); break;
    case 8: wait_vm<8>(); break;
    case 12: wait_vm<12>(); break;
    case 16: wait_vm<16>(); break;
    case 18: wait_vm<18>(); break;
    case 20: wait_vm<20>(); break;
    case 24: wait_vm<24>(); break;
    default: wait_vm<0>(); break;
  }
}
#define BARRIER() __builtin_amdgcn_s_barrier()
__device__ __forceinline__ int bsw3(int k) { return (((k >> 3) & 1) << 2) | (((k >> 1) & 1) << 1); }

template <int CTRL> __device__ __forceinline__ float dpp_f(float v) {
  return __int_as_float(__builtin_amdgcn_update_dpp(0, __float_as_int(v), CTRL, 0xf, 0xf, false));
}
__device__ __forceinline__ float row16_max(float v) {
  v = fmaxf(v, dpp_f<0xB1>(v));
  v = fmaxf(v, dpp_f<0x4E>(v));
  v = fmaxf(v, dpp_f<0x141>(v));
  v = fmaxf(v, dpp_f<0x140>(v));
  return v;
}
__device__ __forceinline__ float row16_min(float v) {
  v = fminf(v, dpp_f<0xB1>(v));
  v = fminf(v, dpp_f<0x4E>(v));
  v = fminf(v, dpp_f<0x141>(v));
  v = fminf(v, dpp_f<0x140>(v));
  return v;
}
__device__ __forceinline__ float row16_sum(float v) {
  v += dpp_f<0xB1>(v);
  v += dpp_f<0x4E>(v);
  v += dpp_f<0x141>(v);
  v += dpp_f<0x140>(v);
  return v;
}
__device__ __forceinline__ float xg_sum(float v) { v += __shfl_xor(v, 16); v += __shfl_xor(v, 32); return v; }
__device__ __forceinline__ float xg_max(float v) { v = fmaxf(v, __shfl_xor(v, 16)); v = fmaxf(v, __shfl_xor(v, 32)); return v; }
__device__ __forceinline__ float xg_min(float v) { v = fminf(v, __shfl_xor(v, 16)); v = fminf(v, __shfl_xor(v, 32)); return v; }
__device__ __forceinline__ float wave_sum(float v) { v = row16_sum(v); return xg_sum(v); }

constexpr int KA_NS = 4;
__global__ __launch_bounds__(256, 1) void k_qkv(const bf16* __restrict__ x, const bf16* __restrict__ wqkv,
                                                const bf16* __restrict__ wphi, bf16* __restrict__ phiq,
                                                float* __restrict__ slab, float* __restrict__ zslab) {
  __shared__ __attribute__((aligned(16))) char smem[65536 + KA_NS * 24576];
  char* const sA = smem;
  char* const sB = smem + 65536;
  const int tid = threadIdx.x, lane = tid & 63;
  const int wid = __builtin_amdgcn_readfirstlane(tid >> 6);
  const int g = lane >> 4, li = lane & 15, q4 = (lane & 15) >> 2, p = lane & 3;
  const int bid = blockIdx.x;
  const int xcd = bid & 7, idx = bid >> 3;
  const int mtile = xcd * 4 + (idx >> 3);
  const int hg = idx & 7;
  const int m0 = mtile * 64;
  const int head = hg * 4 + wid;

  const bf16* bsrc[2];
#pragma unroll
  for (int j = 0; j < 2; ++j) {
    int k = 8 * (2 * wid + j) + (lane >> 3);
    int ch = (lane & 7) ^ bsw3(k);
    bsrc[j] = wqkv + (size_t)k * (3 * D_) + hg * 64 + ch * 8;
  }
  auto issueB = [&](int s) {
    char* base = sB + (s % KA_NS) * 24576;
#pragma unroll
    for (int seg = 0; seg < 3; ++seg)
#pragma unroll
      for (int j = 0; j < 2; ++j)
        glds16(bsrc[j] + (size_t)s * 64 * (3 * D_) + seg * D_, lds_addr(base + seg * 8192 + (2 * wid + j) * 1024));
  };

  u32x4 xr[16];
#pragma unroll
  for (int r = 0; r < 16; ++r)
    xr[r] = *(const u32x4*)(x + (size_t)(m0 + 16 * wid + r) * D_ + lane * 8);
  bf16x4 wp;
#pragma unroll
  for (int j = 0; j < 4; ++j) wp[j] = *(const short*)(wphi + (size_t)head * 256 + (4 * g + j) * 16 + li);

#pragma unroll
  for (int s = 0; s < KA_NS - 1; ++s) issueB(s);

#pragma unroll
  for (int r = 0; r < 16; ++r) {
    float v[8];
#pragma unroll
    for (int j = 0; j < 4; ++j) { v[2 * j] = bf_lo(xr[r][j]); v[2 * j + 1] = bf_hi(xr[r][j]); }
    float s = 0.f;
#pragma unroll
    for (int j = 0; j < 8; ++j) s += v[j];
    const float mean = wave_sum(s) * (1.f / D_);
    float ss = 0.f;
#pragma unroll
    for (int j = 0; j < 8; ++j) { v[j] -= mean; ss += v[j] * v[j]; }
    const float var = wave_sum(ss) * (1.f / D_);
    const float rstd = rsqrtf(var + 1e-5f);
    u32x4 o;
#pragma unroll
    for (int j = 0; j < 4; ++j) o[j] = pk_bf16(v[2 * j] * rstd, v[2 * j + 1] * rstd);
    const int row = 16 * wid + r;
    *(u32x4*)(sA + row * 1024 + (lane >> 4) * 256 + (((lane & 15) ^ (row & 15)) << 4)) = o;
  }

  f32x4 aq[4], ak[4], av[4];
#pragma unroll
  for (int t = 0; t < 4; ++t) { aq[t] = (f32x4){0, 0, 0, 0}; ak[t] = aq[t]; av[t] = aq[t]; }

#pragma unroll
  for (int s = 0; s < 8; ++s) {
    const int last = (s + KA_NS - 2) < 7 ? (s + KA_NS - 2) : 7;
    wait_vm_dyn(6 * (last - s));
    BARRIER();
    if (s + KA_NS - 1 < 8) issueB(s + KA_NS - 1);
    const char* bb = sB + (s % KA_NS) * 24576;
#pragma unroll
    for (int ks = 0; ks < 2; ++ks) {
      bf16x8 af[4];
#pragma unroll
      for (int t = 0; t < 4; ++t) {
        const int row = 16 * t + li, ch = s * 8 + ks * 4 + g;
        af[t] = *(const bf16x8*)(sA + row * 1024 + (ch >> 4) * 256 + (((ch & 15) ^ (row & 15)) << 4));
      }
      bf16x8 bq, bk, bv;
#pragma unroll
      for (int t2 = 0; t2 < 2; ++t2) {
        const int k = 32 * ks + 8 * g + 4 * t2 + q4;
        const int off = k * 128 + (((2 * wid + (p >> 1)) ^ bsw3(k)) << 4) + 8 * (p & 1);
        bf16x4 a = tr_read(bb + off), b = tr_read(bb + 8192 + off), c = tr_read(bb + 16384 + off);
#pragma unroll
        for (int e = 0; e < 4; ++e) { bq[4 * t2 + e] = a[e]; bk[4 * t2 + e] = b[e]; bv[4 * t2 + e] = c[e]; }
      }
#pragma unroll
      for (int t = 0; t < 4; ++t) {
        aq[t] = mfma32(bq, af[t], aq[t]);
        ak[t] = mfma32(bk, af[t], ak[t]);
        av[t] = mfma32(af[t], bv, av[t]);
      }
    }
  }

  const f32x4 zero4 = {0, 0, 0, 0};
  bf16x4 phk_pos[4], phk_neg[4], vb[4];
#pragma unroll
  for (int t = 0; t < 4; ++t) {
    {
      bf16x4 qb;
      unsigned u0 = pk_bf16(aq[t][0], aq[t][1]), u1 = pk_bf16(aq[t][2], aq[t][3]);
      qb[0] = (short)(u0 & 0xffff); qb[1] = (short)(u0 >> 16); qb[2] = (short)(u1 & 0xffff); qb[3] = (short)(u1 >> 16);
      f32x4 pq = mfma16(wp, qb, zero4);
      float pv[4];
#pragma unroll
      for (int r = 0; r < 4; ++r) pv[r] = rbf(pq[r]);
      float mx = fmaxf(fmaxf(pv[0], pv[1]), fmaxf(pv[2], pv[3]));
      float mn = fminf(fminf(pv[0], pv[1]), fminf(pv[2], pv[3]));
      mx = xg_max(mx); mn = xg_min(mn);
      float ep[4], en[4], sp = 0.f, sn = 0.f;
#pragma unroll
      for (int r = 0; r < 4; ++r) { ep[r] = __expf(pv[r] - mx); en[r] = __expf(mn - pv[r]); sp += ep[r]; sn += en[r]; }
      sp = xg_sum(sp); sn = xg_sum(sn);
      const float ip = 1.f / sp, in = 1.f / sn;
      u32x2 op, on;
      op[0] = pk_bf16(ep[0] * ip, ep[1] * ip); op[1] = pk_bf16(ep[2] * ip, ep[3] * ip);
      on[0] = pk_bf16(en[0] * in, en[1] * in); on[1] = pk_bf16(en[2] * in, en[3] * in);
      bf16* dst = phiq + ((size_t)(m0 + 16 * t + li) * H_ + head) * 32 + 4 * g;
      *(u32x2*)dst = op;
      *(u32x2*)(dst + 16) = on;
    }
    {
      bf16x4 kb;
      unsigned u0 = pk_bf16(ak[t][0], ak[t][1]), u1 = pk_bf16(ak[t][2], ak[t][3]);
      kb[0] = (short)(u0 & 0xffff); kb[1] = (short)(u0 >> 16); kb[2] = (short)(u1 & 0xffff); kb[3] = (short)(u1 >> 16);
      f32x4 pk = mfma16(kb, wp, zero4);
      float pp[4], pn[4];
#pragma unroll
      for (int r = 0; r < 4; ++r) {
        const float v = rbf(pk[r]);
        const float mx = row16_max(v), mn = row16_min(v);
        const float e1 = __expf(v - mx), e2 = __expf(mn - v);
        const float s1 = row16_sum(e1), s2 = row16_sum(e2);
        pp[r] = e1 / s1; pn[r] = e2 / s2;
      }
      unsigned a0 = pk_bf16(pp[0], pp[1]), a1 = pk_bf16(pp[2], pp[3]);
      unsigned b0 = pk_bf16(pn[0], pn[1]), b1 = pk_bf16(pn[2], pn[3]);
      phk_pos[t][0] = (short)(a0 & 0xffff); phk_pos[t][1] = (short)(a0 >> 16); phk_pos[t][2] = (short)(a1 & 0xffff); phk_pos[t][3] = (short)(a1 >> 16);
      phk_neg[t][0] = (short)(b0 & 0xffff); phk_neg[t][1] = (short)(b0 >> 16); phk_neg[t][2] = (short)(b1 & 0xffff); phk_neg[t][3] = (short)(b1 >> 16);
    }
    {
      unsigned u0 = pk_bf16(av[t][0], av[t][1]), u1 = pk_bf16(av[t][2], av[t][3]);
      vb[t][0] = (short)(u0 & 0xffff); vb[t][1] = (short)(u0 >> 16); vb[t][2] = (short)(u1 & 0xffff); vb[t][3] = (short)(u1 >> 16);
    }
  }
  f32x4 kvp = zero4, kvn = zero4, zp = zero4, zn = zero4;
  bf16x8 ones;
#pragma unroll
  for (int e = 0; e < 8; ++e) ones[e] = (short)0x3f80;
#pragma unroll
  for (int tp = 0; tp < 2; ++tp) {
    bf16x8 ap, an, bv8;
#pragma unroll
    for (int e = 0; e < 4; ++e) {
      ap[e] = phk_pos[2 * tp][e]; ap[4 + e] = phk_pos[2 * tp + 1][e];
      an[e] = phk_neg[2 * tp][e]; an[4 + e] = phk_neg[2 * tp + 1][e];
      bv8[e] = vb[2 * tp][e]; bv8[4 + e] = vb[2 * tp + 1][e];
    }
    kvp = mfma32(ap, bv8, kvp);
    kvn = mfma32(an, bv8, kvn);
    zp = mfma32(ap, ones, zp);
    zn = mfma32(an, ones, zn);
  }
  float* sl = slab + ((size_t)(mtile * H_ + head) * 32) * 16;
  float* zl = zslab + (size_t)(mtile * H_ + head) * 32;
#pragma unroll
  for (int r = 0; r < 4; ++r) {
    sl[(4 * g + r) * 16 + li] = kvp[r];
    sl[(16 + 4 * g + r) * 16 + li] = kvn[r];
  }
  if (li == 0) {
#pragma unroll
    for (int r = 0; r < 4; ++r) { zl[4 * g + r] = zp[r]; zl[16 + 4 * g + r] = zn[r]; }
  }
}

__global__ __launch_bounds__(256) void k_red(const float* __restrict__ slab, const float* __restrict__ zslab,
                                             bf16* __restrict__ kvt) {
  const int i = blockIdx.x * 256 + threadIdx.x;
  if (i < 32 * 512) {
    float s = 0.f;
#pragma unroll
    for (int m = 0; m < 32; ++m) s += slab[(size_t)m * (32 * 512) + i];
    const int h = i >> 9, f = (i >> 4) & 31, d = i & 15;
    kvt[(h * 16 + d) * 32 + f] = __float2bfloat16(s);
  } else if (i < 32 * 512 + 1024) {
    const int j = i - 32 * 512;
    float s = 0.f;
#pragma unroll
    for (int m = 0; m < 32; ++m) s += zslab[(size_t)m * 1024 + j];
    kvt[16384 + j] = __float2bfloat16(s);
  }
}

__global__ __launch_bounds__(256, 1) void k_attn_o(const bf16* __restrict__ phiq, const bf16* __restrict__ kvt,
                                                   const bf16* __restrict__ wo, const bf16* __restrict__ x,
                                                   bf16* __restrict__ hout, float* __restrict__ stat) {
  __shared__ __attribute__((aligned(16))) char smem[65536 + 34 * 1024];
  char* const sW = smem;
  char* const sK = smem + 65536;
  const int tid = threadIdx.x, lane = tid & 63;
  const int wid = __builtin_amdgcn_readfirstlane(tid >> 6);
  const int g = lane >> 4, li = lane & 15, q4 = (lane & 15) >> 2, p = lane & 3;
  const int bid = blockIdx.x, xcd = bid & 7, idx = bid >> 3;
  const int mtile = xcd * 4 + (idx >> 3), nt = idx & 7;
  const int m0 = mtile * 64, n0 = nt * 64;
  const int tok = m0 + 16 * wid + li;

  {
    const char* src = (const char*)kvt;
#pragma unroll
    for (int j = 0; j < 9; ++j) {
      const int i = wid * 9 + j;
      if (i < 34) glds16(src + i * 1024 + lane * 16, lds_addr(sK + i * 1024));
    }
  }
#pragma unroll
  for (int j = 0; j < 16; ++j) {
    const int i = wid * 16 + j;
    const int k = 8 * i + (lane >> 3);
    const int ch = (lane & 7) ^ (k & 6);
    glds16(wo + (size_t)k * D_ + n0 + ch * 8, lds_addr(sW + i * 1024));
  }
  bf16x8 pf[32];
  {
    const bf16* src = phiq + (size_t)tok * (H_ * 32) + 8 * g;
#pragma unroll
    for (int h = 0; h < 32; ++h) pf[h] = *(const bf16x8*)(src + h * 32);
  }
  u32x2 xres[4];
#pragma unroll
  for (int ni = 0; ni < 4; ++ni) xres[ni] = *(const u32x2*)(x + (size_t)tok * D_ + n0 + 16 * ni + 4 * g);

  wait_vm<0>();
  BARRIER();

  f32x4 acc[4];
#pragma unroll
  for (int ni = 0; ni < 4; ++ni) acc[ni] = (f32x4){0, 0, 0, 0};
  const f32x4 zero4 = {0, 0, 0, 0};
#pragma unroll
  for (int hp = 0; hp < 16; ++hp) {
    bf16x8 ob;
#pragma unroll
    for (int hh = 0; hh < 2; ++hh) {
      const int h = 2 * hp + hh;
      const bf16x8 kf = *(const bf16x8*)(sK + h * 1024 + li * 64 + g * 16);
      const bf16x8 zf = *(const bf16x8*)(sK + 32768 + h * 64 + g * 16);
      const f32x4 num = mfma32(kf, pf[h], zero4);
      const f32x4 den = mfma32(zf, pf[h], zero4);
      const float inv = 1.f / (den[0] + 1e-6f);
      const unsigned u0 = pk_bf16(num[0] * inv, num[1] * inv), u1 = pk_bf16(num[2] * inv, num[3] * inv);
      ob[4 * hh + 0] = (short)(u0 & 0xffff); ob[4 * hh + 1] = (short)(u0 >> 16);
      ob[4 * hh + 2] = (short)(u1 & 0xffff); ob[4 * hh + 3] = (short)(u1 >> 16);
    }
#pragma unroll
    for (int ni = 0; ni < 4; ++ni) {
      bf16x8 wf;
#pragma unroll
      for (int t2 = 0; t2 < 2; ++t2) {
        const int k = 16 * (2 * hp + t2) + 4 * g + q4;
        const bf16x4 a = tr_read(sW + k * 128 + (((2 * ni + (p >> 1)) ^ (k & 6)) << 4) + 8 * (p & 1));
#pragma unroll
        for (int e = 0; e < 4; ++e) wf[4 * t2 + e] = a[e];
      }
      acc[ni] = mfma32(wf, ob, acc[ni]);
    }
  }
  float hv[16];
  float s = 0.f;
#pragma unroll
  for (int ni = 0; ni < 4; ++ni) {
    const float h0 = bf_lo(xres[ni][0]) + acc[ni][0], h1 = bf_hi(xres[ni][0]) + acc[ni][1];
    const float h2 = bf_lo(xres[ni][1]) + acc[ni][2], h3 = bf_hi(xres[ni][1]) + acc[ni][3];
    u32x2 o; o[0] = pk_bf16(h0, h1); o[1] = pk_bf16(h2, h3);
    *(u32x2*)(hout + (size_t)tok * D_ + n0 + 16 * ni + 4 * g) = o;
    hv[4 * ni + 0] = bf_lo(o[0]); hv[4 * ni + 1] = bf_hi(o[0]); hv[4 * ni + 2] = bf_lo(o[1]); hv[4 * ni + 3] = bf_hi(o[1]);
    s += hv[4 * ni] + hv[4 * ni + 1] + hv[4 * ni + 2] + hv[4 * ni + 3];
  }
  const float mean = xg_sum(s) * (1.f / 64.f);
  float m2 = 0.f;
#pragma unroll
  for (int j = 0; j < 16; ++j) { const float d = hv[j] - mean; m2 += d * d; }
  m2 = xg_sum(m2);
  if (g == 0) {
    float* st = stat + ((size_t)tok * 8 + nt) * 2;
    st[0] = mean; st[1] = m2;
  }
}

__global__ __launch_bounds__(256, 1) void k_ffn1(const bf16* __restrict__ h, const float* __restrict__ stat,
                                                 const bf16* __restrict__ wg, const bf16* __restrict__ wu,
                                                 bf16* __restrict__ act) {
  __shared__ __attribute__((aligned(16))) char smem[3 * 16384 + 3 * 32768];
  char* const sA = smem;
  char* const sB = smem + 3 * 16384;
  const int tid = threadIdx.x, lane = tid & 63;
  const int wid = __builtin_amdgcn_readfirstlane(tid >> 6);
  const int g = lane >> 4, li = lane & 15, q4 = (lane & 15) >> 2, p = lane & 3;
  const int bid = blockIdx.x, xcd = bid & 7, idx = bid >> 3;
  const int mtile = (xcd >> 2) * 8 + (idx >> 2), nt = (xcd & 3) * 4 + (idx & 3);
  const int m0 = mtile * 128, f0 = nt * 112;

  const int arow0 = 32 * wid + (lane >> 3);
  float amean[4], arstd[4];
#pragma unroll
  for (int i = 0; i < 4; ++i) {
    const float* st = stat + (size_t)(m0 + arow0 + 8 * i) * 16;
    float mu[8], m2[8];
#pragma unroll
    for (int j = 0; j < 8; ++j) { mu[j] = st[2 * j]; m2[j] = st[2 * j + 1]; }
    float mean = 0.f;
#pragma unroll
    for (int j = 0; j < 8; ++j) mean += mu[j];
    mean *= 0.125f;
    float M2 = 0.f;
#pragma unroll
    for (int j = 0; j < 8; ++j) { const float d = mu[j] - mean; M2 += m2[j] + 64.f * d * d; }
    amean[i] = mean;
    arstd[i] = rsqrtf(M2 * (1.f / D_) + 1e-5f);
  }
  const bf16* asrc = h + (size_t)(m0 + arow0) * D_ + (lane & 7) * 8;
  auto loadA = [&](int s, u32x4 (&r)[4]) {
#pragma unroll
    for (int i = 0; i < 4; ++i) r[i] = *(const u32x4*)(asrc + (size_t)i * 8 * D_ + s * 64);
  };
  auto writeA = [&](int s, const u32x4 (&r)[4]) {
    char* base = sA + (s % 3) * 16384;
#pragma unroll
    for (int i = 0; i < 4; ++i) {
      u32x4 o;
#pragma unroll
      for (int j = 0; j < 4; ++j)
        o[j] = pk_bf16((bf_lo(r[i][j]) - amean[i]) * arstd[i], (bf_hi(r[i][j]) - amean[i]) * arstd[i]);
      const int row = arow0 + 8 * i;
      *(u32x4*)(base + row * 128 + (((lane & 7) ^ ((row >> 1) & 7)) << 4)) = o;
    }
  };
  const bf16* bsrc[8];
#pragma unroll
  for (int j = 0; j < 8; ++j) {
    const int i = wid * 8 + j;
    const int k = 2 * i + (lane >> 5);
    const int seg = (lane >> 4) & 1;
    int ch = (lane & 15) ^ ((bsw3(k) | (k & 1)) << 1);
    if (ch >= 14) ch = 0;
    bsrc[j] = (seg ? wu : wg) + (size_t)k * F_ + f0 + ch * 8;
  }
  auto issueB = [&](int s) {
    char* base = sB + (s % 3) * 32768;
#pragma unroll
    for (int j = 0; j < 8; ++j) glds16(bsrc[j] + (size_t)s * 64 * F_, lds_addr(base + (wid * 8 + j) * 1024));
  };

  f32x4 acc[2][14];
#pragma unroll
  for (int mi = 0; mi < 2; ++mi)
#pragma unroll
    for (int ni = 0; ni < 14; ++ni) acc[mi][ni] = (f32x4){0, 0, 0, 0};

  u32x4 ar[4];
  loadA(0, ar);
  issueB(0);
  issueB(1);
  writeA(0, ar);
  loadA(1, ar);
#pragma unroll
  for (int s = 0; s < 8; ++s) {
    wait_vm<0>();
    BARRIER();
    if (s + 1 < 8) writeA(s + 1, ar);
    if (s + 2 < 8) { loadA(s + 2, ar); issueB(s + 2); }
    const char* ab = sA + (s % 3) * 16384;
    const char* bb = sB + (s % 3) * 32768;
#pragma unroll
    for (int ks = 0; ks < 2; ++ks) {
      bf16x8 af[2];
#pragma unroll
      for (int mi = 0; mi < 2; ++mi) {
        const int row = 32 * wid + 16 * mi + li, ch = ks * 4 + g;
        af[mi] = *(const bf16x8*)(ab + row * 128 + ((ch ^ ((row >> 1) & 7)) << 4));
      }
#pragma unroll
      for (int ni = 0; ni < 14; ++ni) {
        const int seg = ni / 7, c = ni % 7;
        bf16x8 bf;
#pragma unroll
        for (int t2 = 0; t2 < 2; ++t2) {
          const int k = 32 * ks + 8 * g + 4 * t2 + q4;
          const bf16x4 a = tr_read(bb + k * 512 + seg * 256 + (((2 * c + (p >> 1)) ^ ((bsw3(k) | (k & 1)) << 1)) << 4) + 8 * (p & 1));
#pragma unroll
          for (int e = 0; e < 4; ++e) bf[4 * t2 + e] = a[e];
        }
#pragma unroll
        for (int mi = 0; mi < 2; ++mi) acc[mi][ni] = mfma32(bf, af[mi], acc[mi][ni]);
      }
    }
  }
#pragma unroll
  for (int mi = 0; mi < 2; ++mi) {
    bf16* dst = act + (size_t)(m0 + 32 * wid + 16 * mi + li) * F_ + f0 + 4 * g;
#pragma unroll
    for (int c = 0; c < 7; ++c) {
      float o[4];
#pragma unroll
      for (int r = 0; r < 4; ++r) {
        const float gg = acc[mi][c][r], uu = acc[mi][7 + c][r];
        o[r] = gg / (1.f + __expf(-gg)) * uu;
      }
      u32x2 w; w[0] = pk_bf16(o[0], o[1]); w[1] = pk_bf16(o[2], o[3]);
      *(u32x2*)(dst + 16 * c) = w;
    }
  }
}

constexpr int KD_NS = 8;
__global__ __launch_bounds__(256, 1) void k_ffn2(const bf16* __restrict__ act, const bf16* __restrict__ wd,
                                                 const bf16* __restrict__ hres, bf16* __restrict__ out) {
  __shared__ __attribute__((aligned(16))) char smem[KD_NS * 16384];
  const int tid = threadIdx.x, lane = tid & 63;
  const int wid = __builtin_amdgcn_readfirstlane(tid >> 6);
  const int g = lane >> 4, li = lane & 15, q4 = (lane & 15) >> 2, p = lane & 3;
  const int bid = blockIdx.x, xcd = bid & 7, idx = bid >> 3;
  const int mtile = xcd * 4 + (idx >> 3), nt = idx & 7;
  const int m0 = mtile * 64, n0 = nt * 64;
  const int wr = wid >> 1, wc = wid & 1;

  u32x2 hr[2][2];
#pragma unroll
  for (int mi = 0; mi < 2; ++mi)
#pragma unroll
    for (int ni = 0; ni < 2; ++ni)
      hr[mi][ni] = *(const u32x2*)(hres + (size_t)(m0 + wr * 32 + mi * 16 + li) * D_ + n0 + wc * 32 + ni * 16 + 4 * g);

  const bf16* asrc[2];
  const bf16* bsrc[2];
#pragma unroll
  for (int j = 0; j < 2; ++j) {
    const int row = 8 * (2 * wid + j) + (lane >> 3);
    asrc[j] = act + (size_t)(m0 + row) * F_ + (((lane & 7) ^ ((row >> 1) & 7)) * 8);
    bsrc[j] = wd + (size_t)row * D_ + n0 + (((lane & 7) ^ bsw3(row)) * 8);
  }
  auto issue = [&](int s) {
    char* base = smem + (s % KD_NS) * 16384;
#pragma unroll
    for (int j = 0; j < 2; ++j) {
      glds16(asrc[j] + s * 64, lds_addr(base + (2 * wid + j) * 1024));
      glds16(bsrc[j] + (size_t)s * 64 * D_, lds_addr(base + 8192 + (2 * wid + j) * 1024));
    }
  };
  f32x4 acc[2][2];
#pragma unroll
  for (int mi = 0; mi < 2; ++mi)
#pragma unroll
    for (int ni = 0; ni < 2; ++ni) acc[mi][ni] = (f32x4){0, 0, 0, 0};

  constexpr int NK = F_ / 64;
#pragma unroll
  for (int s = 0; s < KD_NS - 1; ++s) issue(s);
#pragma unroll
  for (int s = 0; s < NK; ++s) {
    const int last = (s + KD_NS - 2) < (NK - 1) ? (s + KD_NS - 2) : (NK - 1);
    wait_vm_dyn(4 * (last - s));
    BARRIER();
    if (s + KD_NS - 1 < NK) issue(s + KD_NS - 1);
    const char* base = smem + (s % KD_NS) * 16384;
#pragma unroll
    for (int ks = 0; ks < 2; ++ks) {
      bf16x8 af[2], bf[2];
#pragma unroll
      for (int mi = 0; mi < 2; ++mi) {
        const int row = wr * 32 + mi * 16 + li, ch = ks * 4 + g;
        af[mi] = *(const bf16x8*)(base + row * 128 + ((ch ^ ((row >> 1) & 7)) << 4));
      }
#pragma unroll
      for (int ni = 0; ni < 2; ++ni) {
        const int c = wc * 2 + ni;
#pragma unroll
        for (int t2 = 0; t2 < 2; ++t2) {
          const int k = 32 * ks + 8 * g + 4 * t2 + q4;
          const bf16x4 a = tr_read(base + 8192 + k * 128 + (((2 * c + (p >> 1)) ^ bsw3(k)) << 4) + 8 * (p & 1));
#pragma unroll
          for (int e = 0; e < 4; ++e) bf[ni][4 * t2 + e] = a[e];
        }
      }
#pragma unroll
      for (int mi = 0; mi < 2; ++mi)
#pragma unroll
        for (int ni = 0; ni < 2; ++ni) acc[mi][ni] = mfma32(bf[ni], af[mi], acc[mi][ni]);
    }
  }
#pragma unroll
  for (int mi = 0; mi < 2; ++mi)
#pragma unroll
    for (int ni = 0; ni < 2; ++ni) {
      u32x2 o;
      o[0] = pk_bf16(bf_lo(hr[mi][ni][0]) + acc[mi][ni][0], bf_hi(hr[mi][ni][0]) + acc[mi][ni][1]);
      o[1] = pk_bf16(bf_lo(hr[mi][ni][1]) + acc[mi][ni][2], bf_hi(hr[mi][ni][1]) + acc[mi][ni][3]);
      *(u32x2*)(out + (size_t)(m0 + wr * 32 + mi * 16 + li) * D_ + n0 + wc * 32 + ni * 16 + 4 * g) = o;
    }
}

extern "C" void kernel_launch(void* const* d_in, const int* in_sizes, int n_in, void* d_out, int out_size,
                              void* d_ws, size_t ws_size, hipStream_t stream) {
  const bf16* x = (const bf16*)d_in[0];
  const bf16* wqkv = (const bf16*)d_in[1];
  const bf16* wphi = (const bf16*)d_in[2];
  const bf16* wo = (const bf16*)d_in[3];
  const bf16* wg = (const bf16*)d_in[5];
  const bf16* wu = (const bf16*)d_in[6];
  const bf16* wd = (const bf16*)d_in[7];
  char* ws = (char*)d_ws;
  bf16* phiq = (bf16*)(ws + WS_PHIQ);
  float* slab = (float*)(ws + WS_SLAB);
  float* zslab = (float*)(ws + WS_ZSLAB);
  bf16* kvt = (bf16*)(ws + WS_KVT);
  bf16* hbuf = (bf16*)(ws + WS_H);
  float* stat = (float*)(ws + WS_STAT);
  bf16* act = (bf16*)(ws + WS_ACT);
  k_qkv<<<256, 256, 0, stream>>>(x, wqkv, wphi, phiq, slab, zslab);
  k_red<<<68, 256, 0, stream>>>(slab, zslab, kvt);
  k_attn_o<<<256, 256, 0, stream>>>(phiq, kvt, wo, x, hbuf, stat);
  k_ffn1<<<256, 256, 0, stream>>>(hbuf, stat, wg, wu, act);
  k_ffn2<<<256, 256, 0, stream>>>(act, wd, hbuf, (bf16*)d_out);
}
```

```cpp
#include <hip/hip_runtime.h>
#include <hip/hip_bf16.h>
#include <stdint.h>

typedef __hip_bfloat16 bf16;
typedef __attribute__((ext_vector_type(8))) short bf16x8;
typedef __attribute__((ext_vector_type(4))) short bf16x4;
typedef __attribute__((ext_vector_type(4))) float f32x4;
typedef __attribute__((ext_vector_type(4))) unsigned int u32x4;
typedef __attribute__((ext_vector_type(2))) unsigned int u32x2;
typedef __attribute__((ext_vector_type(8))) __bf16 bfv8;
typedef __attribute__((ext_vector_type(4))) __bf16 bfv4;
#define LAS __attribute__((address_space(3)))

constexpr int S_ = 2048, D_ = 512, H_ = 32, F_ = 1792;

constexpr size_t WS_PHIQ = 0;
constexpr size_t WS_SLAB = 4u << 20;
constexpr size_t WS_ZSLAB = WS_SLAB + (size_t)32 * 32 * 32 * 16 * 4;
constexpr size_t WS_KVT = 8u << 20;
constexpr size_t WS_CSUM = WS_KVT + 65536;
constexpr size_t WS_H = 9u << 20;
constexpr size_t WS_STAT = 11u << 20;
constexpr size_t WS_ACT = 12u << 20;

__device__ __forceinline__ f32x4 mfma32(bf16x8 a, bf16x8 b, f32x4 c) {
  return __builtin_amdgcn_mfma_f32_16x16x32_bf16(__builtin_bit_cast(bfv8, a), __builtin_bit_cast(bfv8, b), c, 0, 0, 0);
}
__device__ __forceinline__ f32x4 mfma16(bf16x4 a, bf16x4 b, f32x4 c) {
  return __builtin_amdgcn_mfma_f32_16x16x16bf16_1k(a, b, c, 0, 0, 0);
}
__device__ __forceinline__ unsigned pk_bf16(float lo, float hi) {
  typedef __attribute__((ext_vector_type(2))) __bf16 bf2;
  typedef __attribute__((ext_vector_type(2))) float f2;
  f2 v = {lo, hi};
  bf2 r = __builtin_convertvector(v, bf2);
  return __builtin_bit_cast(unsigned, r);
}
__device__ __forceinline__ float rbf(float x) {
  unsigned u = pk_bf16(x, 0.f);
  return __uint_as_float(u << 16);
}
__device__ __forceinline__ float bf_lo(unsigned u) { return __uint_as_float(u << 16); }
__device__ __forceinline__ float bf_hi(unsigned u) { return __uint_as_float(u & 0xffff0000u); }

__device__ __forceinline__ void glds16(const void* g, unsigned lds_base) {
  unsigned keep;
  const unsigned b = __builtin_amdgcn_readfirstlane(lds_base);
  asm volatile("s_mov_b32 %0, m0\n\ts_mov_b32 m0, %2\n\ts_nop 0\n\tglobal_load_lds_dwordx4 %1, off\n\ts_mov_b32 m0, %0"
               : "=&s"(keep) : "v"(g), "s"(b) : "memory");
}
__device__ __forceinline__ unsigned lds_addr(const void* p) { return (unsigned)(size_t)(LAS const char*)p; }
__device__ __forceinline__ bf16x4 tr_read(const char* p) {
  return __builtin_amdgcn_ds_read_tr16_b64_v4i16((LAS bf16x4*)p);
}
template <int N> __device__ __forceinline__ void wait_vm() { asm volatile("s_waitcnt vmcnt(%0)" ::"n"(N) : "memory"); }
__device__ __forceinline__ void wait_vm_dyn(int n) {
  switch (n) {
    case 0: wait_vm<0>(); break;
    case 1: wait_vm<1>(); break;
    case 2: wait_vm<2>(); break;
    case 3: wait_vm<3>(); break;
    case 4: wait_vm<4>(); break;
    case 6: wait_vm<6>(); break;
    case 8: wait_vm<8>(); break;
    case 12: wait_vm<12>(); break;
    case 16: wait_vm<16>(); break;
    case 18: wait_vm<18>(); break;
    case 20: wait_vm<20>(); break;
    case 24: wait_vm<24>(); break;
    default: wait_vm<0>(); break;
  }
}
#define BARRIER() __builtin_amdgcn_s_barrier()
#define STAMP(i) do { } while (0)
__device__ __forceinline__ int bsw3(int k) { return (((k >> 3) & 1) << 2) | (((k >> 1) & 1) << 1); }

template <int CTRL> __device__ __forceinline__ float dpp_f(float v) {
  return __int_as_float(__builtin_amdgcn_update_dpp(0, __float_as_int(v), CTRL, 0xf, 0xf, false));
}
__device__ __forceinline__ float row16_max(float v) {
  v = fmaxf(v, dpp_f<0xB1>(v));
  v = fmaxf(v, dpp_f<0x4E>(v));
  v = fmaxf(v, dpp_f<0x141>(v));
  v = fmaxf(v, dpp_f<0x140>(v));
  return v;
}
__device__ __forceinline__ float row16_min(float v) {
  v = fminf(v, dpp_f<0xB1>(v));
  v = fminf(v, dpp_f<0x4E>(v));
  v = fminf(v, dpp_f<0x141>(v));
  v = fminf(v, dpp_f<0x140>(v));
  return v;
}
__device__ __forceinline__ float row16_sum(float v) {
  v += dpp_f<0xB1>(v);
  v += dpp_f<0x4E>(v);
  v += dpp_f<0x141>(v);
  v += dpp_f<0x140>(v);
  return v;
}
__device__ __forceinline__ float xg_sum(float v) {
  auto r = __builtin_amdgcn_permlane16_swap(__float_as_int(v), __float_as_int(v), false, false);
  v = __int_as_float(r[0]) + __int_as_float(r[1]);
  auto t = __builtin_amdgcn_permlane32_swap(__float_as_int(v), __float_as_int(v), false, false);
  return __int_as_float(t[0]) + __int_as_float(t[1]);
}
__device__ __forceinline__ float xg_max(float v) {
  auto r = __builtin_amdgcn_permlane16_swap(__float_as_int(v), __float_as_int(v), false, false);
  v = fmaxf(__int_as_float(r[0]), __int_as_float(r[1]));
  auto t = __builtin_amdgcn_permlane32_swap(__float_as_int(v), __float_as_int(v), false, false);
  return fmaxf(__int_as_float(t[0]), __int_as_float(t[1]));
}
__device__ __forceinline__ float xg_min(float v) {
  auto r = __builtin_amdgcn_permlane16_swap(__float_as_int(v), __float_as_int(v), false, false);
  v = fminf(__int_as_float(r[0]), __int_as_float(r[1]));
  auto t = __builtin_amdgcn_permlane32_swap(__float_as_int(v), __float_as_int(v), false, false);
  return fminf(__int_as_float(t[0]), __int_as_float(t[1]));
}
__device__ __forceinline__ float quad_sum(float v) { v += dpp_f<0xB1>(v); v += dpp_f<0x4E>(v); return v; }
__device__ __forceinline__ bf16x4 pack4(float a, float b, float c, float d) {
  const unsigned u0 = pk_bf16(a, b), u1 = pk_bf16(c, d);
  bf16x4 r; r[0] = (short)(u0 & 0xffff); r[1] = (short)(u0 >> 16); r[2] = (short)(u1 & 0xffff); r[3] = (short)(u1 >> 16);
  return r;
}
__device__ __forceinline__ float wave_sum(float v) { v = row16_sum(v); return xg_sum(v); }

constexpr int KA_NS = 4;
__global__ __launch_bounds__(256, 1) void k_qkv(const bf16* __restrict__ x, const bf16* __restrict__ wqkv,
                                                const bf16* __restrict__ wphi, bf16* __restrict__ phiq,
                                                float* __restrict__ slab, float* __restrict__ zslab) {
  __shared__ __attribute__((aligned(16))) char smem[65536 + KA_NS * 24576];
  char* const sA = smem;
  char* const sB = smem + 65536;
  const int tid = threadIdx.x, lane = tid & 63;
  constexpr int KID = 0; (void)KID;
  const int wid = __builtin_amdgcn_readfirstlane(tid >> 6);
  const int g = lane >> 4, li = lane & 15, q4 = (lane & 15) >> 2, p = lane & 3;
  const int bid = blockIdx.x;
  const int xcd = bid & 7, idx = bid >> 3;
  const int mtile = xcd * 4 + (idx >> 3);
  const int hg = idx & 7;
  const int m0 = mtile * 64;
  const int head = hg * 4 + wid;
  STAMP(0);
#pragma unroll
  for (int r = 0; r < 16; ++r) {
    const int row = 16 * wid + r;
    const int ch = (lane & 48) + (((lane & 15) ^ (row & 15) ^ (((lane >> 4) & 3) << 2)) & 15);
    glds16(x + (size_t)(m0 + row) * D_ + ch * 8, lds_addr(sA + row * 1024));
  }
  const bf16* bsrc[2];
#pragma unroll
  for (int j = 0; j < 2; ++j) {
    int k = 8 * (2 * wid + j) + (lane >> 3);
    int ch = (lane & 7) ^ bsw3(k);
    bsrc[j] = wqkv + (size_t)k * (3 * D_) + hg * 64 + ch * 8;
  }
  auto issueB = [&](int s) {
    char* base = sB + (s % KA_NS) * 24576;
#pragma unroll
    for (int seg = 0; seg < 3; ++seg)
#pragma unroll
      for (int j = 0; j < 2; ++j)
        glds16(bsrc[j] + (size_t)s * 64 * (3 * D_) + seg * D_, lds_addr(base + seg * 8192 + (2 * wid + j) * 1024));
  };
#pragma unroll
  for (int s = 0; s < KA_NS - 1; ++s) issueB(s);
  bf16x4 wp;
#pragma unroll
  for (int j = 0; j < 4; ++j) wp[j] = *(const short*)(wphi + (size_t)head * 256 + (4 * g + j) * 16 + li);
  wait_vm<18>();
  BARRIER();
  STAMP(1);
  float smean, srstd;
  {
    const int row = tid >> 2, qd = tid & 3;
    u32x4 v[16];
#pragma unroll
    for (int j = 0; j < 16; ++j)
      v[j] = *(const u32x4*)(sA + row * 1024 + qd * 256 + (((j ^ (row & 15) ^ (qd << 2)) & 15) << 4));
    float sm = 0.f;
#pragma unroll
    for (int j = 0; j < 16; ++j)
#pragma unroll
      for (int e = 0; e < 4; ++e) sm += bf_lo(v[j][e]) + bf_hi(v[j][e]);
    smean = quad_sum(sm) * (1.f / D_);
    float ss = 0.f;
#pragma unroll
    for (int j = 0; j < 16; ++j)
#pragma unroll
      for (int e = 0; e < 4; ++e) {
        const float a = bf_lo(v[j][e]) - smean, c = bf_hi(v[j][e]) - smean;
        ss += a * a + c * c;
      }
    srstd = rsqrtf(quad_sum(ss) * (1.f / D_) + 1e-5f);
  }
  STAMP(2);
  f32x4 aq[4], ak[4], av[4], cq, ck, cv;
  const f32x4 zero4 = {0, 0, 0, 0};
#pragma unroll
  for (int t = 0; t < 4; ++t) { aq[t] = zero4; ak[t] = zero4; av[t] = zero4; }
  cq = zero4; ck = zero4; cv = zero4;
  bf16x8 ones;
#pragma unroll
  for (int e = 0; e < 8; ++e) ones[e] = (short)0x3f80;

#pragma unroll
  for (int s = 0; s < 8; ++s) {
    const int last = (s + KA_NS - 2) < 7 ? (s + KA_NS - 2) : 7;
    wait_vm_dyn(6 * (last - s));
    BARRIER();
    if (s + KA_NS - 1 < 8) issueB(s + KA_NS - 1);
    if (s == 0) STAMP(3);
    if (s == 4) STAMP(4);
    const char* bb = sB + (s % KA_NS) * 24576;
#pragma unroll
    for (int ks = 0; ks < 2; ++ks) {
      bf16x8 af[4];
#pragma unroll
      for (int t = 0; t < 4; ++t) {
        const int row = 16 * t + li, ch = s * 8 + ks * 4 + g;
        af[t] = *(const bf16x8*)(sA + row * 1024 + (ch >> 4) * 256 + ((((ch & 15) ^ (row & 15) ^ (((ch >> 4) & 3) << 2)) & 15) << 4));
      }
      bf16x8 bq, bk, bv;
#pragma unroll
      for (int t2 = 0; t2 < 2; ++t2) {
        const int k = 32 * ks + 8 * g + 4 * t2 + q4;
        const int off = k * 128 + (((2 * wid + (p >> 1)) ^ bsw3(k)) << 4) + 8 * (p & 1);
        bf16x4 a = tr_read(bb + off), b = tr_read(bb + 8192 + off), c = tr_read(bb + 16384 + off);
#pragma unroll
        for (int e = 0; e < 4; ++e) { bq[4 * t2 + e] = a[e]; bk[4 * t2 + e] = b[e]; bv[4 * t2 + e] = c[e]; }
      }
#pragma unroll
      for (int t = 0; t < 4; ++t) {
        aq[t] = mfma32(bq, af[t], aq[t]);
        ak[t] = mfma32(bk, af[t], ak[t]);
        av[t] = mfma32(af[t], bv, av[t]);
      }
      cq = mfma32(bq, ones, cq);
      ck = mfma32(bk, ones, ck);
      cv = mfma32(ones, bv, cv);
    }
  }
  BARRIER();
  if ((tid & 3) == 0) { float* st = (float*)sA + (tid >> 2) * 2; st[0] = smean; st[1] = srstd; }
  __syncthreads();
  STAMP(5);
  const float* st = (const float*)sA;
  bf16x4 phk_pos[4], phk_neg[4], vb[4];
  float pv[4][4], pkv[4][4];
#pragma unroll
  for (int t = 0; t < 4; ++t) {
    const float mu = st[(16 * t + li) * 2], rs = st[(16 * t + li) * 2 + 1];
    const f32x4 pq = mfma16(wp, pack4(rs * (aq[t][0] - mu * cq[0]), rs * (aq[t][1] - mu * cq[1]),
                                      rs * (aq[t][2] - mu * cq[2]), rs * (aq[t][3] - mu * cq[3])), zero4);
    const f32x4 pk = mfma16(pack4(rs * (ak[t][0] - mu * ck[0]), rs * (ak[t][1] - mu * ck[1]),
                                  rs * (ak[t][2] - mu * ck[2]), rs * (ak[t][3] - mu * ck[3])), wp, zero4);
    float vv[4];
#pragma unroll
    for (int r = 0; r < 4; ++r) {
      const int tk = 16 * t + 4 * g + r;
      vv[r] = st[tk * 2 + 1] * (av[t][r] - st[tk * 2] * cv[r]);
      pv[t][r] = rbf(pq[r]);
      pkv[t][r] = rbf(pk[r]);
    }
    vb[t] = pack4(vv[0], vv[1], vv[2], vv[3]);
  }
  float mxq[4], mnq[4];
#pragma unroll
  for (int t = 0; t < 4; ++t) {
    mxq[t] = xg_max(fmaxf(fmaxf(pv[t][0], pv[t][1]), fmaxf(pv[t][2], pv[t][3])));
    mnq[t] = xg_min(fminf(fminf(pv[t][0], pv[t][1]), fminf(pv[t][2], pv[t][3])));
  }
#pragma unroll
  for (int t = 0; t < 4; ++t) {
    float ep[4], en[4], sp = 0.f, sn = 0.f;
#pragma unroll
    for (int r = 0; r < 4; ++r) { ep[r] = __expf(pv[t][r] - mxq[t]); en[r] = __expf(mnq[t] - pv[t][r]); sp += ep[r]; sn += en[r]; }
    sp = xg_sum(sp); sn = xg_sum(sn);
    const float ip = __builtin_amdgcn_rcpf(sp), in = __builtin_amdgcn_rcpf(sn);
    u32x2 op, on;
    op[0] = pk_bf16(ep[0] * ip, ep[1] * ip); op[1] = pk_bf16(ep[2] * ip, ep[3] * ip);
    on[0] = pk_bf16(en[0] * in, en[1] * in); on[1] = pk_bf16(en[2] * in, en[3] * in);
    char* dst = (char*)phiq + ((size_t)(mtile * 4 + t) * H_ + head) * 1024 + li * 16 + (g & 1) * 8;
    *(u32x2*)(dst + (g >> 1) * 256) = op;
    *(u32x2*)(dst + (2 + (g >> 1)) * 256) = on;
  }
#pragma unroll
  for (int t = 0; t < 4; ++t) {
    float pp[4], pn[4];
#pragma unroll
    for (int r = 0; r < 4; ++r) {
      const float v = pkv[t][r];
      const float mx = row16_max(v), mn = row16_min(v);
      const float e1 = __expf(v - mx), e2 = __expf(mn - v);
      const float s1 = row16_sum(e1), s2 = row16_sum(e2);
      pp[r] = e1 * __builtin_amdgcn_rcpf(s1); pn[r] = e2 * __builtin_amdgcn_rcpf(s2);
    }
    phk_pos[t] = pack4(pp[0], pp[1], pp[2], pp[3]);
    phk_neg[t] = pack4(pn[0], pn[1], pn[2], pn[3]);
  }
  f32x4 kvp = zero4, kvn = zero4, zp = zero4, zn = zero4;
#pragma unroll
  for (int tp = 0; tp < 2; ++tp) {
    bf16x8 ap, an, bv8;
#pragma unroll
    for (int e = 0; e < 4; ++e) {
      ap[e] = phk_pos[2 * tp][e]; ap[4 + e] = phk_pos[2 * tp + 1][e];
      an[e] = phk_neg[2 * tp][e]; an[4 + e] = phk_neg[2 * tp + 1][e];
      bv8[e] = vb[2 * tp][e]; bv8[4 + e] = vb[2 * tp + 1][e];
    }
    kvp = mfma32(ap, bv8, kvp);
    kvn = mfma32(an, bv8, kvn);
    zp = mfma32(ap, ones, zp);
    zn = mfma32(an, ones, zn);
  }
  float* sl = slab + ((size_t)(mtile * H_ + head) * 32) * 16;
  float* zl = zslab + (size_t)(mtile * H_ + head) * 32;
#pragma unroll
  for (int r = 0; r < 4; ++r) {
    sl[(4 * g + r) * 16 + li] = kvp[r];
    sl[(16 + 4 * g + r) * 16 + li] = kvn[r];
  }
  if (li == 0) {
#pragma unroll
    for (int r = 0; r < 4; ++r) { zl[4 * g + r] = zp[r]; zl[16 + 4 * g + r] = zn[r]; }
  }
  STAMP(6);
}

__global__ __launch_bounds__(256) void k_red(const float* __restrict__ slab, const float* __restrict__ zslab,
                                             bf16* __restrict__ kvt, const bf16* __restrict__ wg,
                                             const bf16* __restrict__ wu, float* __restrict__ csum) {
  __shared__ float red[32][65];
  if (blockIdx.x < 68) {
    const int i = blockIdx.x * 256 + threadIdx.x;
    if (i < 32 * 512) {
      float s = 0.f;
#pragma unroll
      for (int m = 0; m < 32; ++m) s += slab[(size_t)m * (32 * 512) + i];
      const int h = i >> 9, f = (i >> 4) & 31, d = i & 15;
      kvt[(h * 16 + d) * 32 + f] = __float2bfloat16(s);
    } else if (i < 32 * 512 + 1024) {
      const int j = i - 32 * 512;
      float s = 0.f;
#pragma unroll
      for (int m = 0; m < 32; ++m) s += zslab[(size_t)m * 1024 + j];
      kvt[16384 + j] = __float2bfloat16(s);
    }
    return;
  }
  const int cb = blockIdx.x - 68;
  const bf16* w = (cb < 28 ? wg : wu) + (cb % 28) * 64 + (threadIdx.x & 7) * 8;
  const int kg = threadIdx.x >> 3;
  u32x4 v[16];
#pragma unroll
  for (int j = 0; j < 16; ++j) v[j] = *(const u32x4*)(w + (size_t)(kg * 16 + j) * F_);
  float a[8] = {0, 0, 0, 0, 0, 0, 0, 0};
#pragma unroll
  for (int j = 0; j < 16; ++j)
#pragma unroll
    for (int e = 0; e < 4; ++e) { a[2 * e] += bf_lo(v[j][e]); a[2 * e + 1] += bf_hi(v[j][e]); }
#pragma unroll
  for (int e = 0; e < 8; ++e) red[kg][(threadIdx.x & 7) * 8 + e] = a[e];
  __syncthreads();
  if (threadIdx.x < 64) {
    float s = 0.f;
#pragma unroll
    for (int k = 0; k < 32; ++k) s += red[k][threadIdx.x];
    csum[cb * 64 + threadIdx.x] = s;
  }
}

__global__ __launch_bounds__(256, 1) void k_attn_o(const bf16* __restrict__ phiq, const bf16* __restrict__ kvt,
                                                   const bf16* __restrict__ wo, const bf16* __restrict__ x,
                                                   bf16* __restrict__ hout, float* __restrict__ stat) {
  __shared__ __attribute__((aligned(16))) char smem[65536 + 34 * 1024];
  char* const sW = smem;
  char* const sK = smem + 65536;
  constexpr int KID = 1; (void)KID;
  const int tid = threadIdx.x, lane = tid & 63;
  const int wid = __builtin_amdgcn_readfirstlane(tid >> 6);
  const int g = lane >> 4, li = lane & 15, q4 = (lane & 15) >> 2, p = lane & 3;
  const int bid = blockIdx.x, xcd = bid & 7, idx = bid >> 3;
  const int mtile = xcd * 4 + (idx >> 3), nt = idx & 7;
  const int m0 = mtile * 64, n0 = nt * 64;
  const int tok = m0 + 16 * wid + li;
  STAMP(0);

  {
    const char* src = (const char*)kvt;
#pragma unroll
    for (int j = 0; j < 9; ++j) {
      const int i = wid * 9 + j;
      if (i < 34) glds16(src + i * 1024 + lane * 16, lds_addr(sK + i * 1024));
    }
  }
#pragma unroll
  for (int j = 0; j < 16; ++j) {
    const int i = wid * 16 + j;
    const int k = 8 * i + (lane >> 3);
    const int ch = (lane & 7) ^ (k & 6);
    glds16(wo + (size_t)k * D_ + n0 + ch * 8, lds_addr(sW + i * 1024));
  }
  bf16x8 pf[32];
  {
    const char* src = (const char*)phiq + (size_t)(mtile * 4 + wid) * H_ * 1024 + lane * 16;
#pragma unroll
    for (int h = 0; h < 32; ++h) pf[h] = *(const bf16x8*)(src + h * 1024);
  }
  u32x2 xres[4];
#pragma unroll
  for (int ni = 0; ni < 4; ++ni) xres[ni] = *(const u32x2*)(x + (size_t)tok * D_ + n0 + 16 * ni + 4 * g);

  wait_vm<0>();
  BARRIER();

  f32x4 acc[4];
  STAMP(1);
#pragma unroll
  for (int ni = 0; ni < 4; ++ni) acc[ni] = (f32x4){0, 0, 0, 0};
  const f32x4 zero4 = {0, 0, 0, 0};
  bf16x8 ob[16];
#pragma unroll
  for (int hp = 0; hp < 16; ++hp) {
#pragma unroll
    for (int hh = 0; hh < 2; ++hh) {
      const int h = 2 * hp + hh;
      const bf16x8 kf = *(const bf16x8*)(sK + h * 1024 + li * 64 + g * 16);
      const bf16x8 zf = *(const bf16x8*)(sK + 32768 + h * 64 + g * 16);
      const f32x4 num = mfma32(kf, pf[h], zero4);
      const f32x4 den = mfma32(zf, pf[h], zero4);
      const float inv = 1.f / (den[0] + 1e-6f);
      const bf16x4 o4 = pack4(num[0] * inv, num[1] * inv, num[2] * inv, num[3] * inv);
#pragma unroll
      for (int e = 0; e < 4; ++e) ob[hp][4 * hh + e] = o4[e];
    }
  }
#pragma unroll
  for (int hp = 0; hp < 16; ++hp) {
#pragma unroll
    for (int ni = 0; ni < 4; ++ni) {
      bf16x8 wf;
#pragma unroll
      for (int t2 = 0; t2 < 2; ++t2) {
        const int k = 16 * (2 * hp + t2) + 4 * g + q4;
        const bf16x4 a = tr_read(sW + k * 128 + (((2 * ni + (p >> 1)) ^ (k & 6)) << 4) + 8 * (p & 1));
#pragma unroll
        for (int e = 0; e < 4; ++e) wf[4 * t2 + e] = a[e];
      }
      acc[ni] = mfma32(wf, ob[hp], acc[ni]);
    }
  }
  STAMP(2);
  float hv[16];
  float s = 0.f;
#pragma unroll
  for (int ni = 0; ni < 4; ++ni) {
    const float h0 = bf_lo(xres[ni][0]) + acc[ni][0], h1 = bf_hi(xres[ni][0]) + acc[ni][1];
    const float h2 = bf_lo(xres[ni][1]) + acc[ni][2], h3 = bf_hi(xres[ni][1]) + acc[ni][3];
    u32x2 o; o[0] = pk_bf16(h0, h1); o[1] = pk_bf16(h2, h3);
    *(u32x2*)(hout + (size_t)tok * D_ + n0 + 16 * ni + 4 * g) = o;
    hv[4 * ni + 0] = bf_lo(o[0]); hv[4 * ni + 1] = bf_hi(o[0]); hv[4 * ni + 2] = bf_lo(o[1]); hv[4 * ni + 3] = bf_hi(o[1]);
    s += hv[4 * ni] + hv[4 * ni + 1] + hv[4 * ni + 2] + hv[4 * ni + 3];
  }
  const float mean = xg_sum(s) * (1.f / 64.f);
  float m2 = 0.f;
#pragma unroll
  for (int j = 0; j < 16; ++j) { const float d = hv[j] - mean; m2 += d * d; }
  m2 = xg_sum(m2);
  if (g == 0) {
    float* st = stat + ((size_t)tok * 8 + nt) * 2;
    st[0] = mean; st[1] = m2;
  }
  STAMP(3);
}

constexpr int KC_NS = 6;
__global__ __launch_bounds__(256, 1) void k_ffn1(const bf16* __restrict__ h, const float* __restrict__ stat,
                                                 const float* __restrict__ csum, const bf16* __restrict__ wg,
                                                 const bf16* __restrict__ wu, bf16* __restrict__ act) {
  __shared__ __attribute__((aligned(16))) char smem[KC_NS * 24576];
  constexpr int KID = 2; (void)KID;
  const int tid = threadIdx.x, lane = tid & 63;
  const int wid = __builtin_amdgcn_readfirstlane(tid >> 6);
  const int g = lane >> 4, li = lane & 15, q4 = (lane & 15) >> 2, p = lane & 3;
  const int bid = blockIdx.x, xcd = bid & 7, idx = bid >> 3;
  const int mtile = (xcd >> 2) * 8 + (idx >> 2), nt = (xcd & 3) * 4 + (idx & 3);
  const int m0 = mtile * 128, f0 = nt * 112;
  STAMP(0);
  const bf16* asrc[2];
#pragma unroll
  for (int j = 0; j < 2; ++j) {
    const int row = 16 * (2 * wid + j) + (lane >> 2);
    asrc[j] = h + (size_t)(m0 + row) * D_ + (((lane & 3) ^ ((-(row >> 2)) & 3)) * 8);
  }
  const bf16* bsrc[4];
#pragma unroll
  for (int j = 0; j < 4; ++j) {
    const int k = 2 * (wid * 4 + j) + (lane >> 5);
    const int seg = (lane >> 4) & 1;
    int ch = (lane & 15) ^ ((bsw3(k) | (k & 1)) << 1);
    if (ch >= 14) ch = 0;
    bsrc[j] = (seg ? wu : wg) + (size_t)k * F_ + f0 + ch * 8;
  }
  auto issue = [&](int s) {
    char* base = smem + (s % KC_NS) * 24576;
#pragma unroll
    for (int j = 0; j < 2; ++j) glds16(asrc[j] + s * 32, lds_addr(base + (2 * wid + j) * 1024));
#pragma unroll
    for (int j = 0; j < 4; ++j) glds16(bsrc[j] + (size_t)s * 32 * F_, lds_addr(base + 8192 + (wid * 4 + j) * 1024));
  };
#pragma unroll
  for (int s = 0; s < KC_NS - 1; ++s) issue(s);

  float amean[2], arstd[2];
#pragma unroll
  for (int mi = 0; mi < 2; ++mi) {
    const float* st = stat + (size_t)(m0 + 32 * wid + 16 * mi + li) * 16;
    float mu[8], m2[8];
#pragma unroll
    for (int j = 0; j < 8; ++j) { mu[j] = st[2 * j]; m2[j] = st[2 * j + 1]; }
    float mean = 0.f;
#pragma unroll
    for (int j = 0; j < 8; ++j) mean += mu[j];
    mean *= 0.125f;
    float M2 = 0.f;
#pragma unroll
    for (int j = 0; j < 8; ++j) { const float d = mu[j] - mean; M2 += m2[j] + 64.f * d * d; }
    amean[mi] = mean;
    arstd[mi] = rsqrtf(M2 * (1.f / D_) + 1e-5f);
  }
  f32x4 cs[14];
#pragma unroll
  for (int ni = 0; ni < 14; ++ni) cs[ni] = *(const f32x4*)(csum + (ni / 7) * F_ + f0 + (ni % 7) * 16 + 4 * g);

  f32x4 acc[2][14];
#pragma unroll
  for (int mi = 0; mi < 2; ++mi)
#pragma unroll
    for (int ni = 0; ni < 14; ++ni) acc[mi][ni] = (f32x4){0, 0, 0, 0};

  constexpr int NK = 16;
#pragma unroll
  for (int s = 0; s < NK; ++s) {
    const int last = (s + KC_NS - 2) < (NK - 1) ? (s + KC_NS - 2) : (NK - 1);
    wait_vm_dyn(6 * (last - s));
    BARRIER();
    if (s + KC_NS - 1 < NK) issue(s + KC_NS - 1);
    if (s == 0) STAMP(2);
    if (s == 8) STAMP(3);
    const char* ab = smem + (s % KC_NS) * 24576;
    const char* bb = ab + 8192;
    bf16x8 af[2];
#pragma unroll
    for (int mi = 0; mi < 2; ++mi) {
      const int row = 32 * wid + 16 * mi + li;
      af[mi] = *(const bf16x8*)(ab + row * 64 + ((g ^ ((-(row >> 2)) & 3)) << 4));
    }
#pragma unroll
    for (int ni = 0; ni < 14; ++ni) {
      const int seg = ni / 7, c = ni % 7;
      bf16x8 bf;
#pragma unroll
      for (int t2 = 0; t2 < 2; ++t2) {
        const int k = 8 * g + 4 * t2 + q4;
        const bf16x4 a = tr_read(bb + k * 512 + seg * 256 + (((2 * c + (p >> 1)) ^ ((bsw3(k) | (k & 1)) << 1)) << 4) + 8 * (p & 1));
#pragma unroll
        for (int e = 0; e < 4; ++e) bf[4 * t2 + e] = a[e];
      }
#pragma unroll
      for (int mi = 0; mi < 2; ++mi) acc[mi][ni] = mfma32(bf, af[mi], acc[mi][ni]);
    }
  }
  STAMP(4);
#pragma unroll
  for (int mi = 0; mi < 2; ++mi) {
    bf16* dst = act + (size_t)(m0 + 32 * wid + 16 * mi + li) * F_ + f0 + 4 * g;
#pragma unroll
    for (int c = 0; c < 7; ++c) {
      float o[4];
#pragma unroll
      for (int r = 0; r < 4; ++r) {
        const float gg = arstd[mi] * (acc[mi][c][r] - amean[mi] * cs[c][r]);
        const float uu = arstd[mi] * (acc[mi][7 + c][r] - amean[mi] * cs[7 + c][r]);
        o[r] = gg * __builtin_amdgcn_rcpf(1.f + __expf(-gg)) * uu;
      }
      u32x2 w; w[0] = pk_bf16(o[0], o[1]); w[1] = pk_bf16(o[2], o[3]);
      *(u32x2*)(dst + 16 * c) = w;
    }
  }
  STAMP(5);
}

constexpr int KD_NS = 8;
__global__ __launch_bounds__(256, 1) void k_ffn2(const bf16* __restrict__ act, const bf16* __restrict__ wd,
                                                 const bf16* __restrict__ hres, bf16* __restrict__ out) {
  __shared__ __attribute__((aligned(16))) char smem[KD_NS * 16384];
  constexpr int KID = 3; (void)KID;
  const int tid = threadIdx.x, lane = tid & 63;
  const int wid = __builtin_amdgcn_readfirstlane(tid >> 6);
  const int g = lane >> 4, li = lane & 15, q4 = (lane & 15) >> 2, p = lane & 3;
  const int bid = blockIdx.x, xcd = bid & 7, idx = bid >> 3;
  const int mtile = xcd * 4 + (idx >> 3), nt = idx & 7;
  const int m0 = mtile * 64, n0 = nt * 64;
  const int wr = wid >> 1, wc = wid & 1;
  STAMP(0);
  u32x2 hr[2][2];
#pragma unroll
  for (int mi = 0; mi < 2; ++mi)
#pragma unroll
    for (int ni = 0; ni < 2; ++ni)
      hr[mi][ni] = *(const u32x2*)(hres + (size_t)(m0 + wr * 32 + mi * 16 + li) * D_ + n0 + wc * 32 + ni * 16 + 4 * g);

  const bf16* asrc[2];
  const bf16* bsrc[2];
#pragma unroll
  for (int j = 0; j < 2; ++j) {
    const int row = 8 * (2 * wid + j) + (lane >> 3);
    asrc[j] = act + (size_t)(m0 + row) * F_ + (((lane & 7) ^ ((row >> 1) & 7)) * 8);
    bsrc[j] = wd + (size_t)row * D_ + n0 + (((lane & 7) ^ bsw3(row)) * 8);
  }
  auto issue = [&](int s) {
    char* base = smem + (s % KD_NS) * 16384;
#pragma unroll
    for (int j = 0; j < 2; ++j) {
      glds16(asrc[j] + s * 64, lds_addr(base + (2 * wid + j) * 1024));
      glds16(bsrc[j] + (size_t)s * 64 * D_, lds_addr(base + 8192 + (2 * wid + j) * 1024));
    }
  };
  f32x4 acc[2][2];
#pragma unroll
  for (int mi = 0; mi < 2; ++mi)
#pragma unroll
    for (int ni = 0; ni < 2; ++ni) acc[mi][ni] = (f32x4){0, 0, 0, 0};

  constexpr int NK = F_ / 64;
#pragma unroll
  for (int s = 0; s < KD_NS - 1; ++s) issue(s);
#pragma unroll
  for (int s = 0; s < NK; ++s) {
    const int last = (s + KD_NS - 2) < (NK - 1) ? (s + KD_NS - 2) : (NK - 1);
    wait_vm_dyn(4 * (last - s));
    BARRIER();
    if (s == 0) STAMP(1);
    if (s == 4) STAMP(2);
    if (s == 14) STAMP(3);
    if (s == 27) STAMP(4);
    if (s + KD_NS - 1 < NK) issue(s + KD_NS - 1);
    const char* base = smem + (s % KD_NS) * 16384;
#pragma unroll
    for (int ks = 0; ks < 2; ++ks) {
      bf16x8 af[2], bf[2];
#pragma unroll
      for (int mi = 0; mi < 2; ++mi) {
        const int row = wr * 32 + mi * 16 + li, ch = ks * 4 + g;
        af[mi] = *(const bf16x8*)(base + row * 128 + ((ch ^ ((row >> 1) & 7)) << 4));
      }
#pragma unroll
      for (int ni = 0; ni < 2; ++ni) {
        const int c = wc * 2 + ni;
#pragma unroll
        for (int t2 = 0; t2 < 2; ++t2) {
          const int k = 32 * ks + 8 * g + 4 * t2 + q4;
          const bf16x4 a = tr_read(base + 8192 + k * 128 + (((2 * c + (p >> 1)) ^ bsw3(k)) << 4) + 8 * (p & 1));
#pragma unroll
          for (int e = 0; e < 4; ++e) bf[ni][4 * t2 + e] = a[e];
        }
      }
#pragma unroll
      for (int mi = 0; mi < 2; ++mi)
#pragma unroll
        for (int ni = 0; ni < 2; ++ni) acc[mi][ni] = mfma32(bf[ni], af[mi], acc[mi][ni]);
    }
  }
#pragma unroll
  for (int mi = 0; mi < 2; ++mi)
#pragma unroll
    for (int ni = 0; ni < 2; ++ni) {
      u32x2 o;
      o[0] = pk_bf16(bf_lo(hr[mi][ni][0]) + acc[mi][ni][0], bf_hi(hr[mi][ni][0]) + acc[mi][ni][1]);
      o[1] = pk_bf16(bf_lo(hr[mi][ni][1]) + acc[mi][ni][2], bf_hi(hr[mi][ni][1]) + acc[mi][ni][3]);
      *(u32x2*)(out + (size_t)(m0 + wr * 32 + mi * 16 + li) * D_ + n0 + wc * 32 + ni * 16 + 4 * g) = o;
    }
  STAMP(5);
}

extern "C" void kernel_launch(void* const* d_in, const int* in_sizes, int n_in, void* d_out, int out_size,
                              void* d_ws, size_t ws_size, hipStream_t stream) {
  const bf16* x = (const bf16*)d_in[0];
  const bf16* wqkv = (const bf16*)d_in[1];
  const bf16* wphi = (const bf16*)d_in[2];
  const bf16* wo = (const bf16*)d_in[3];
  const bf16* wg = (const bf16*)d_in[5];
  const bf16* wu = (const bf16*)d_in[6];
  const bf16* wd = (const bf16*)d_in[7];
  char* ws = (char*)d_ws;
  bf16* phiq = (bf16*)(ws + WS_PHIQ);
  float* slab = (float*)(ws + WS_SLAB);
  float* zslab = (float*)(ws + WS_ZSLAB);
  bf16* kvt = (bf16*)(ws + WS_KVT);
  bf16* hbuf = (bf16*)(ws + WS_H);
  float* stat = (float*)(ws + WS_STAT);
  bf16* act = (bf16*)(ws + WS_ACT);
  k_qkv<<<256, 256, 0, stream>>>(x, wqkv, wphi, phiq, slab, zslab);
  float* csum = (float*)(ws + WS_CSUM);
  k_red<<<68 + 56, 256, 0, stream>>>(slab, zslab, kvt, wg, wu, csum);
  k_attn_o<<<256, 256, 0, stream>>>(phiq, kvt, wo, x, hbuf, stat);
  k_ffn1<<<256, 256, 0, stream>>>(hbuf, stat, csum, wg, wu, act);
  k_ffn2<<<256, 256, 0, stream>>>(act, wd, hbuf, (bf16*)d_out);
}
```
